```python
import torch

def _softmax(x, axis=-1):
    return torch.softmax(x, dim=axis)
def _gelu(x):
    return torch.nn.functional.gelu(x, approximate="tanh")
def _silu(x):
    return torch.nn.functional.silu(x)
def _sigmoid(x):
    return torch.sigmoid(x)
def _layernorm(x, axis=-1, eps=1e-5):
    m = x.mean(dim=axis, keepdim=True)
    v = x.var(dim=axis, keepdim=True, unbiased=False)
    return (x - m) / torch.sqrt(v + eps)
def _scatter_add(tgt, idx, src, axis=0):
    return tgt.clone().index_add_(axis, idx, src)
def _segment_sum(src, seg, n):
    out = torch.zeros((n,) + src.shape[1:], dtype=src.dtype)
    return out.index_add_(0, seg, src)
def _pad(x, pw):
    flat = tuple(v for pair in reversed(pw) for v in pair)
    return torch.nn.functional.pad(x, flat)

kernel_name = 'standalone__channel_attn_se__vision'

def setup_inputs():

    _w = (lambda a: (lambda t: t.to(torch.bfloat16) if t.is_floating_point() else t)(torch.as_tensor(a)))
    H = W = 512
    C = 96
    r = max(C // 16, 4)
    return {
        "x": _w((lambda *s: torch.randn(*s, dtype=torch.bfloat16) * 3.2)(1, H, W, C)),
        "w_down": _w((lambda *s: torch.randn(*s, dtype=torch.bfloat16) * 12.8)(C, r)),
        "w_up": _w((lambda *s: torch.randn(*s, dtype=torch.bfloat16) * 3.2)(r, C)),
    }

def reference(x, w_down, w_up):
    s = x.mean(axis=(1, 2))
    h = _silu(torch.einsum('bc,cr->br', s, w_down))
    g = _sigmoid(torch.einsum('br,rc->bc', h, w_up))
    return x * g[:, None, None, :]
```

```cpp
#include <hip/hip_runtime.h>
#include <hip/hip_bf16.h>
#include <stdint.h>

typedef unsigned int u32;
typedef u32 u32x4 __attribute__((ext_vector_type(4)));
typedef float f32x4 __attribute__((ext_vector_type(4)));

constexpr int C = 96, R = 6;
constexpr int NPIX = 512 * 512;
constexpr int NT = 768;
constexpr int NWG = 256;
constexpr int CH = 16;
static_assert((long)NWG * NT * CH * 8 == (long)NPIX * C, "tiling");

__device__ __forceinline__ float bf_lo(u32 v) { return __uint_as_float(v << 16); }
__device__ __forceinline__ float bf_hi(u32 v) { return __uint_as_float(v & 0xffff0000u); }
__device__ __forceinline__ float rbf(float f) { return __bfloat162float(__float2bfloat16(f)); }
__device__ __forceinline__ u32 pack2(float a, float b) {
    return (u32)__bfloat16_as_ushort(__float2bfloat16(a)) | ((u32)__bfloat16_as_ushort(__float2bfloat16(b)) << 16);
}

__global__ void __launch_bounds__(NT, 3) se_reduce(const u32x4* __restrict__ x, float* __restrict__ part) {
    __shared__ float red[NT * 8];
    const int t = threadIdx.x, wg = blockIdx.x;
    const u32x4* p = x + (size_t)wg * (NT * CH) + t;
    u32x4 v[CH];
#pragma unroll
    for (int k = 0; k < CH; ++k) v[k] = p[k * NT];
    float a[8] = {0, 0, 0, 0, 0, 0, 0, 0};
#pragma unroll
    for (int k = 0; k < CH; ++k) {
#pragma unroll
        for (int j = 0; j < 4; ++j) { a[2 * j] += bf_lo(v[k][j]); a[2 * j + 1] += bf_hi(v[k][j]); }
    }
    *(f32x4*)&red[t * 8] = (f32x4){a[0], a[1], a[2], a[3]};
    *(f32x4*)&red[t * 8 + 4] = (f32x4){a[4], a[5], a[6], a[7]};
    __syncthreads();
    __shared__ float red2[4 * 96];
    if (t < 384) {
        const int c = t % 96, q = t / 96;
        float s = 0;
#pragma unroll
        for (int r = 0; r < 16; ++r) s += red[(q * 16 + r) * 96 + c];
        red2[q * 96 + c] = s;
    }
    __syncthreads();
    if (t < 96) part[wg * 96 + t] = (red2[t] + red2[96 + t]) + (red2[192 + t] + red2[288 + t]);
}

__global__ void __launch_bounds__(NT, 3) se_scale(const u32x4* __restrict__ x, const float* __restrict__ part,
                                                  const __hip_bfloat16* __restrict__ wd, const __hip_bfloat16* __restrict__ wu,
                                                  u32x4* __restrict__ out) {
    __shared__ float red[32 * 96];
    __shared__ float sh_s[96];
    __shared__ float sh_h[8];
    const int t = threadIdx.x, wg = blockIdx.x;
    f32x4 ps[8];
    {
        const f32x4* pp = (const f32x4*)part + (t / 24) * 8 * 24 + (t % 24);
#pragma unroll
        for (int k = 0; k < 8; ++k) ps[k] = pp[k * 24];
    }
    const u32x4* p = x + (size_t)wg * (NT * CH) + t;
    u32x4 v[CH];
#pragma unroll
    for (int k = 0; k < CH; ++k) v[k] = p[k * NT];
    {
        f32x4 s = ((ps[0] + ps[1]) + (ps[2] + ps[3])) + ((ps[4] + ps[5]) + (ps[6] + ps[7]));
        *(f32x4*)&red[(t / 24) * 96 + (t % 24) * 4] = s;
    }
    __syncthreads();
    if (t < 96) {
        float s = 0;
#pragma unroll
        for (int r = 0; r < 32; ++r) s += red[r * 96 + t];
        sh_s[t] = rbf(s * (1.0f / NPIX));
    }
    __syncthreads();
    const int wave = t >> 6, lane = t & 63;
    if (wave < R) {
        float acc = sh_s[lane] * __bfloat162float(wd[lane * R + wave]);
        if (lane < 32) acc += sh_s[64 + lane] * __bfloat162float(wd[(64 + lane) * R + wave]);
#pragma unroll
        for (int m = 32; m >= 1; m >>= 1) acc += __shfl_xor(acc, m);
        if (lane == 0) {
            float hp = rbf(acc);
            sh_h[wave] = rbf(hp / (1.0f + __expf(-hp)));
        }
    }
    __syncthreads();
    float g[8];
    {
        const int c0 = (t % 12) * 8;
#pragma unroll
        for (int j = 0; j < 8; ++j) {
            float acc = 0;
#pragma unroll
            for (int r = 0; r < R; ++r) acc += sh_h[r] * __bfloat162float(wu[r * C + c0 + j]);
            acc = rbf(acc);
            g[j] = rbf(1.0f / (1.0f + __expf(-acc)));
        }
    }
    u32x4* o = out + (size_t)wg * (NT * CH) + t;
#pragma unroll
    for (int k = 0; k < CH; ++k) {
        u32x4 r;
#pragma unroll
        for (int j = 0; j < 4; ++j) r[j] = pack2(bf_lo(v[k][j]) * g[2 * j], bf_hi(v[k][j]) * g[2 * j + 1]);
        o[k * NT] = r;
    }
}

extern "C" void kernel_launch(void* const* d_in, const int* in_sizes, int n_in,
                              void* d_out, int out_size, void* d_ws, size_t ws_size,
                              hipStream_t stream) {
    const u32x4* x = (const u32x4*)d_in[0];
    const __hip_bfloat16* wd = (const __hip_bfloat16*)d_in[1];
    const __hip_bfloat16* wu = (const __hip_bfloat16*)d_in[2];
    float* part = (float*)d_ws;
    se_reduce<<<NWG, NT, 0, stream>>>(x, part);
    se_scale<<<NWG, NT, 0, stream>>>(x, part, wd, wu, (u32x4*)d_out);
}
```

```cpp
#include <hip/hip_runtime.h>
#include <hip/hip_bf16.h>
#include <stdint.h>

typedef unsigned int u32;
typedef u32 u32x4 __attribute__((ext_vector_type(4)));
typedef float f32x4 __attribute__((ext_vector_type(4)));
#define GAS __attribute__((address_space(1)))
typedef GAS unsigned gu32;
#define RLX_AGENT __ATOMIC_RELAXED, __HIP_MEMORY_SCOPE_AGENT

constexpr int C = 96, R = 6;
constexpr int NPIX = 512 * 512;
constexpr int NT = 768;
constexpr int NWG = 256;
constexpr int CH = 16;
static_assert((long)NWG * NT * CH * 8 == (long)NPIX * C, "tiling");
constexpr int CTL_BYTES = 4096;
constexpr unsigned kSpinCap = 1u << 22;

__device__ __forceinline__ float bf_lo(u32 v) { return __uint_as_float(v << 16); }
__device__ __forceinline__ float bf_hi(u32 v) { return __uint_as_float(v & 0xffff0000u); }
__device__ __forceinline__ float rbf(float f) { return __bfloat162float(__float2bfloat16(f)); }
__device__ __forceinline__ u32 pack2(float a, float b) {
    return (u32)__bfloat16_as_ushort(__float2bfloat16(a)) | ((u32)__bfloat16_as_ushort(__float2bfloat16(b)) << 16);
}

__global__ void __launch_bounds__(NT, 3) se_fused(const u32x4* __restrict__ x, float* part, unsigned* ctl,
                                                  const __hip_bfloat16* __restrict__ wd, const __hip_bfloat16* __restrict__ wu,
                                                  u32x4* __restrict__ out) {
    __shared__ float red[NT * 8];
    __shared__ float red2[4 * 96];
    __shared__ float sh_s[96];
    __shared__ float sh_h[8];
    const int t = threadIdx.x, wg = blockIdx.x;
    const int wave = t >> 6, lane = t & 63;
    unsigned long long* st = (unsigned long long*)(ctl + 1024*64) + wg * 8;
    if (t == 0) st[0] = __builtin_amdgcn_s_memrealtime();
    const __amdgpu_buffer_rsrc_t xrs = __builtin_amdgcn_make_buffer_rsrc((void*)x, 0, NPIX * C * 2, 0x00020000);
    u32x4 v[CH];
    {
        const unsigned xoff = (wg * NT * CH + t) * 16;
#pragma unroll
        for (int k = 0; k < CH; ++k) v[k] = __builtin_bit_cast(u32x4, __builtin_amdgcn_raw_buffer_load_b128(xrs, xoff + k * NT * 16, 0, 2));
    }
    {
        float a[8] = {0, 0, 0, 0, 0, 0, 0, 0};
#pragma unroll
        for (int k = 0; k < CH; ++k) {
#pragma unroll
            for (int j = 0; j < 4; ++j) { a[2 * j] += bf_lo(v[k][j]); a[2 * j + 1] += bf_hi(v[k][j]); }
        }
        *(f32x4*)&red[t * 8] = (f32x4){a[0], a[1], a[2], a[3]};
        *(f32x4*)&red[t * 8 + 4] = (f32x4){a[4], a[5], a[6], a[7]};
    }
    __syncthreads();
    if (t == 0) st[1] = __builtin_amdgcn_s_memrealtime();
    if (t < 384) {
        const int c = t % 96, q = t / 96;
        float s = 0;
#pragma unroll
        for (int r = 0; r < 16; ++r) s += red[(q * 16 + r) * 96 + c];
        red2[q * 96 + c] = s;
    }
    __syncthreads();
    const __amdgpu_buffer_rsrc_t prs = __builtin_amdgcn_make_buffer_rsrc(part, 0, NWG * 96 * 4, 0x00020000);
    if (wave == 0) {
        if (lane < 24) {
            f32x4 s = *(f32x4*)&red2[lane * 4] + *(f32x4*)&red2[96 + lane * 4] + (*(f32x4*)&red2[192 + lane * 4] + *(f32x4*)&red2[288 + lane * 4]);
            __builtin_amdgcn_raw_buffer_store_b128(__builtin_bit_cast(u32x4, s), prs, (unsigned)((wg * 96 + lane * 4) * 4), 0, 16);
        }
        asm volatile("s_waitcnt vmcnt(0)" ::: "memory");
        if (lane == 0) __hip_atomic_fetch_add((gu32*)ctl, 1u, RLX_AGENT);
        if (t == 0) st[2] = __builtin_amdgcn_s_memrealtime();
        for (unsigned spins = 0;; ) {
            if ((unsigned)__builtin_amdgcn_readfirstlane(__hip_atomic_load((gu32*)ctl, RLX_AGENT)) >= (unsigned)NWG) break;
            __builtin_amdgcn_s_sleep(1);
            if (++spins > kSpinCap) break;
        }
    }
    __syncthreads();
    if (t == 0) st[3] = __builtin_amdgcn_s_memrealtime();
    {
        f32x4 ps[8];
        const unsigned off = ((t / 24) * 8 * 24 + (t % 24)) * 16;
#pragma unroll
        for (int k = 0; k < 8; ++k) ps[k] = __builtin_bit_cast(f32x4, __builtin_amdgcn_raw_buffer_load_b128(prs, off + k * 24 * 16, 0, 16));
        f32x4 s = ((ps[0] + ps[1]) + (ps[2] + ps[3])) + ((ps[4] + ps[5]) + (ps[6] + ps[7]));
        *(f32x4*)&red[(t / 24) * 96 + (t % 24) * 4] = s;
    }
    __syncthreads();
    if (t < 96) {
        float s = 0;
#pragma unroll
        for (int r = 0; r < 32; ++r) s += red[r * 96 + t];
        sh_s[t] = rbf(s * (1.0f / NPIX));
    }
    __syncthreads();
    if (t == 0) st[4] = __builtin_amdgcn_s_memrealtime();
    if (wave < R) {
        float acc = sh_s[lane] * __bfloat162float(wd[lane * R + wave]);
        if (lane < 32) acc += sh_s[64 + lane] * __bfloat162float(wd[(64 + lane) * R + wave]);
#pragma unroll
        for (int m = 32; m >= 1; m >>= 1) acc += __shfl_xor(acc, m);
        if (lane == 0) {
            float hp = rbf(acc);
            sh_h[wave] = rbf(hp / (1.0f + __expf(-hp)));
        }
    }
    __syncthreads();
    float g[8];
    {
        const int c0 = (t % 12) * 8;
#pragma unroll
        for (int j = 0; j < 8; ++j) {
            float acc = 0;
#pragma unroll
            for (int r = 0; r < R; ++r) acc += sh_h[r] * __bfloat162float(wu[r * C + c0 + j]);
            acc = rbf(acc);
            g[j] = rbf(1.0f / (1.0f + __expf(-acc)));
        }
    }
    if (t == 0) st[5] = __builtin_amdgcn_s_memrealtime();
    u32x4* o = out + (size_t)wg * (NT * CH) + t;
#pragma unroll
    for (int k = 0; k < CH; ++k) {
        u32x4 r;
#pragma unroll
        for (int j = 0; j < 4; ++j) r[j] = pack2(bf_lo(v[k][j]) * g[2 * j], bf_hi(v[k][j]) * g[2 * j + 1]);
        o[k * NT] = r;
    }
    asm volatile("s_waitcnt vmcnt(0)" ::: "memory");
    if (t == 0) { st[6] = __builtin_amdgcn_s_memrealtime();
      if ((wg & 15) == 0) printf("STAMP %d %llu %llu %llu %llu %llu %llu %llu\n", wg, st[0], st[1]-st[0], st[2]-st[0], st[3]-st[0], st[4]-st[0], st[5]-st[0], st[6]-st[0]); }
}

extern "C" void kernel_launch(void* const* d_in, const int* in_sizes, int n_in,
                              void* d_out, int out_size, void* d_ws, size_t ws_size,
                              hipStream_t stream) {
    const u32x4* x = (const u32x4*)d_in[0];
    const __hip_bfloat16* wd = (const __hip_bfloat16*)d_in[1];
    const __hip_bfloat16* wu = (const __hip_bfloat16*)d_in[2];
    unsigned* ctl = (unsigned*)d_ws;
    float* part = (float*)((char*)d_ws + CTL_BYTES);
    hipMemsetAsync(ctl, 0, CTL_BYTES, stream);
    se_fused<<<NWG, NT, 0, stream>>>(x, part, ctl, wd, wu, (u32x4*)d_out);
}
```

```cpp
#include <hip/hip_runtime.h>
#include <hip/hip_bf16.h>
#include <stdint.h>

typedef unsigned int u32;
typedef unsigned long long u64;
typedef u32 u32x4 __attribute__((ext_vector_type(4)));
typedef float f32x4 __attribute__((ext_vector_type(4)));
#define GAS __attribute__((address_space(1)))
typedef GAS u64 gu64;
#define RLX_AGENT __ATOMIC_RELAXED, __HIP_MEMORY_SCOPE_AGENT

constexpr int C = 96, R = 6;
constexpr int NPIX = 512 * 512;
constexpr int NT = 768;
constexpr int NWG = 256;
constexpr int CH = 16;
static_assert((long)NWG * NT * CH * 8 == (long)NPIX * C, "tiling");
constexpr int GSZ = 16;
constexpr int NGRP = NWG / GSZ;
constexpr int L1_WORDS = NWG * C;
constexpr int L2_WORDS = NGRP * C;
constexpr int CTL_BYTES = (L1_WORDS + L2_WORDS) * 8;
constexpr unsigned kSpinCap = 1u << 20;
constexpr u64 TAG = 1ull << 32;

__device__ __forceinline__ float bf_lo(u32 v) { return __uint_as_float(v << 16); }
__device__ __forceinline__ float bf_hi(u32 v) { return __uint_as_float(v & 0xffff0000u); }
__device__ __forceinline__ float rbf(float f) { return __bfloat162float(__float2bfloat16(f)); }
__device__ __forceinline__ u32 pack2(float a, float b) {
    return (u32)__bfloat16_as_ushort(__float2bfloat16(a)) | ((u32)__bfloat16_as_ushort(__float2bfloat16(b)) << 16);
}
__device__ __forceinline__ float wait_granule(u64* p) {
    u64 x;
    for (unsigned spins = 0;; ) {
        x = __hip_atomic_load((gu64*)p, RLX_AGENT);
        if ((x >> 32) == 1ull) break;
        __builtin_amdgcn_s_sleep(1);
        if (++spins > kSpinCap) break;
    }
    return __uint_as_float((u32)x);
}

__global__ void __launch_bounds__(NT, 3) se_fused(const u32x4* __restrict__ x, u64* gran,
                                                  const __hip_bfloat16* __restrict__ wd, const __hip_bfloat16* __restrict__ wu,
                                                  u32x4* __restrict__ out) {
    __shared__ float red[NT * 8];
    __shared__ float red2[4 * 96];
    __shared__ float sh_wu[R * C];
    __shared__ float sh_h[8];
    const int t = threadIdx.x, wg = blockIdx.x;
    const int wave = t >> 6, lane = t & 63;
    const __amdgpu_buffer_rsrc_t xrs = __builtin_amdgcn_make_buffer_rsrc((void*)x, 0, NPIX * C * 2, 0x00020000);
    u32x4 v[CH];
    {
        const unsigned xoff = (wg * NT * CH + t) * 16;
#pragma unroll
        for (int k = 0; k < CH; ++k) v[k] = __builtin_bit_cast(u32x4, __builtin_amdgcn_raw_buffer_load_b128(xrs, xoff + k * NT * 16, 0, 18));
    }
    float wd0 = 0.f, wd1 = 0.f;
    if (wave < R) {
        wd0 = __bfloat162float(wd[lane * R + wave]);
        if (lane < 32) wd1 = __bfloat162float(wd[(64 + lane) * R + wave]);
    }
    if (t < R * C) sh_wu[t] = __bfloat162float(wu[t]);
    {
        float a[8] = {0, 0, 0, 0, 0, 0, 0, 0};
#pragma unroll
        for (int k = 0; k < CH; ++k) {
#pragma unroll
            for (int j = 0; j < 4; ++j) { a[2 * j] += bf_lo(v[k][j]); a[2 * j + 1] += bf_hi(v[k][j]); }
        }
        *(f32x4*)&red[t * 8] = (f32x4){a[0], a[1], a[2], a[3]};
        *(f32x4*)&red[t * 8 + 4] = (f32x4){a[4], a[5], a[6], a[7]};
    }
#pragma unroll
    for (int k = 0; k < CH; ++k) asm volatile("" : "+v"(v[k]));
    __syncthreads();
    if (t < 384) {
        const int c = t % 96, q = t / 96;
        float s = 0;
#pragma unroll
        for (int r = 0; r < 16; ++r) s += red[(q * 16 + r) * 96 + c];
        red2[q * 96 + c] = s;
    }
    __syncthreads();
    if (t < 96) {
        const float s = (red2[t] + red2[96 + t]) + (red2[192 + t] + red2[288 + t]);
        __hip_atomic_store((gu64*)(gran + wg * C + t), TAG | __float_as_uint(s), RLX_AGENT);
    }
    if ((wg % GSZ) == 0) {
        const float a0 = wait_granule(gran + wg * C + t);
        const float a1 = wait_granule(gran + wg * C + 768 + t);
        red[t] = a0; red[768 + t] = a1;
        __syncthreads();
        if (t < 96) {
            float s = 0;
#pragma unroll
            for (int r = 0; r < GSZ; ++r) s += red[r * 96 + t];
            __hip_atomic_store((gu64*)(gran + L1_WORDS + (wg / GSZ) * C + t), TAG | __float_as_uint(s), RLX_AGENT);
        }
        __syncthreads();
    }
    {
        const float a0 = wait_granule(gran + L1_WORDS + t);
        const float a1 = wait_granule(gran + L1_WORDS + 768 + t);
        red[t] = a0; red[768 + t] = a1;
    }
    __syncthreads();
    if (wave < R) {
        float s0 = 0, s1 = 0;
#pragma unroll
        for (int r = 0; r < NGRP; ++r) { s0 += red[r * 96 + lane]; if (lane < 32) s1 += red[r * 96 + 64 + lane]; }
        float acc = rbf(s0 * (1.0f / NPIX)) * wd0 + rbf(s1 * (1.0f / NPIX)) * wd1;
#pragma unroll
        for (int m = 32; m >= 1; m >>= 1) acc += __shfl_xor(acc, m);
        if (lane == 0) {
            float hp = rbf(acc);
            sh_h[wave] = rbf(hp / (1.0f + __expf(-hp)));
        }
    }
    __syncthreads();
    float g[8];
    {
        const int c0 = (t % 12) * 8;
#pragma unroll
        for (int j = 0; j < 8; ++j) {
            float acc = 0;
#pragma unroll
            for (int r = 0; r < R; ++r) acc += sh_h[r] * sh_wu[r * C + c0 + j];
            acc = rbf(acc);
            g[j] = rbf(1.0f / (1.0f + __expf(-acc)));
        }
    }
    u32x4* o = out + (size_t)wg * (NT * CH) + t;
#pragma unroll
    for (int k = 0; k < CH; ++k) {
        u32x4 r;
#pragma unroll
        for (int j = 0; j < 4; ++j) r[j] = pack2(bf_lo(v[k][j]) * g[2 * j], bf_hi(v[k][j]) * g[2 * j + 1]);
        o[k * NT] = r;
    }
}

extern "C" void kernel_launch(void* const* d_in, const int* in_sizes, int n_in,
                              void* d_out, int out_size, void* d_ws, size_t ws_size,
                              hipStream_t stream) {
    const u32x4* x = (const u32x4*)d_in[0];
    const __hip_bfloat16* wd = (const __hip_bfloat16*)d_in[1];
    const __hip_bfloat16* wu = (const __hip_bfloat16*)d_in[2];
    u64* gran = (u64*)d_ws;
    hipMemsetAsync(gran, 0, CTL_BYTES, stream);
    se_fused<<<NWG, NT, 0, stream>>>(x, gran, wd, wu, (u32x4*)d_out);
}
```

```cpp
#include <hip/hip_runtime.h>
#include <hip/hip_bf16.h>
#include <stdint.h>

typedef unsigned int u32;
typedef unsigned long long u64;
typedef u32 u32x4 __attribute__((ext_vector_type(4)));
typedef float f32x4 __attribute__((ext_vector_type(4)));
#define GAS __attribute__((address_space(1)))
typedef GAS u64 gu64;
#define RLX_AGENT __ATOMIC_RELAXED, __HIP_MEMORY_SCOPE_AGENT

constexpr int C = 96, R = 6;
constexpr int NPIX = 512 * 512;
constexpr int NT = 768;
constexpr int NWG = 256;
constexpr int CH = 16;
static_assert((long)NWG * NT * CH * 8 == (long)NPIX * C, "tiling");
constexpr int GSZ = 16;
constexpr int NGRP = NWG / GSZ;
constexpr int L1_WORDS = NWG * C;
constexpr int L2_WORDS = NGRP * C;
constexpr int CTL_BYTES = (L1_WORDS + L2_WORDS) * 8;
constexpr unsigned kSpinCap = 1u << 20;

__device__ __forceinline__ float bf_lo(u32 v) { return __uint_as_float(v << 16); }
__device__ __forceinline__ float bf_hi(u32 v) { return __uint_as_float(v & 0xffff0000u); }
__device__ __forceinline__ float rbf(float f) { return __bfloat162float(__float2bfloat16(f)); }
__device__ __forceinline__ u32 pack2(float a, float b) {
    return (u32)__bfloat16_as_ushort(__float2bfloat16(a)) | ((u32)__bfloat16_as_ushort(__float2bfloat16(b)) << 16);
}
__device__ __forceinline__ float wait_granule(u64* p, u32 tag) {
    u64 x;
    for (unsigned spins = 0;; ) {
        x = __hip_atomic_load((gu64*)p, RLX_AGENT);
        if ((u32)(x >> 32) == tag) break;
        __builtin_amdgcn_s_sleep(1);
        if (++spins > kSpinCap) break;
    }
    return __uint_as_float((u32)x);
}

__global__ void __launch_bounds__(NT, 3) se_fused(const u32x4* __restrict__ x, u64* gran,
                                                  const __hip_bfloat16* __restrict__ wd, const __hip_bfloat16* __restrict__ wu,
                                                  u32x4* __restrict__ out) {
    __shared__ float red[NT * 8];
    __shared__ float red2[4 * 96];
    __shared__ float sh_wu[R * C];
    __shared__ float sh_h[8];
    const int t = threadIdx.x, wg = blockIdx.x;
    const int wave = t >> 6, lane = t & 63;
    u64* epoch_p = gran + L1_WORDS + L2_WORDS;
    const u32 tag = (u32)__hip_atomic_load((gu64*)epoch_p, RLX_AGENT) + 1u;
    const u64 TAG = (u64)tag << 32;
    const __amdgpu_buffer_rsrc_t xrs = __builtin_amdgcn_make_buffer_rsrc((void*)x, 0, NPIX * C * 2, 0x00020000);
    u32x4 v[CH];
    {
        const unsigned xoff = (wg * NT * CH + t) * 16;
#pragma unroll
        for (int k = 0; k < CH; ++k) v[k] = __builtin_bit_cast(u32x4, __builtin_amdgcn_raw_buffer_load_b128(xrs, xoff + k * NT * 16, 0, 0));
    }
    float wd0 = 0.f, wd1 = 0.f;
    if (wave < R) {
        wd0 = __bfloat162float(wd[lane * R + wave]);
        if (lane < 32) wd1 = __bfloat162float(wd[(64 + lane) * R + wave]);
    }
    if (t < R * C) sh_wu[t] = __bfloat162float(wu[t]);
    {
        float a[8] = {0, 0, 0, 0, 0, 0, 0, 0};
#pragma unroll
        for (int k = 0; k < CH; ++k) {
#pragma unroll
            for (int j = 0; j < 4; ++j) { a[2 * j] += bf_lo(v[k][j]); a[2 * j + 1] += bf_hi(v[k][j]); }
        }
        *(f32x4*)&red[t * 8] = (f32x4){a[0], a[1], a[2], a[3]};
        *(f32x4*)&red[t * 8 + 4] = (f32x4){a[4], a[5], a[6], a[7]};
    }
#pragma unroll
    for (int k = 0; k < CH; ++k) asm volatile("" : "+v"(v[k]));
    __syncthreads();
    if (t < 384) {
        const int c = t % 96, q = t / 96;
        float s = 0;
#pragma unroll
        for (int r = 0; r < 16; ++r) s += red[(q * 16 + r) * 96 + c];
        red2[q * 96 + c] = s;
    }
    __syncthreads();
    if (t < 96) {
        const float s = (red2[t] + red2[96 + t]) + (red2[192 + t] + red2[288 + t]);
        __hip_atomic_store((gu64*)(gran + wg * C + t), TAG | __float_as_uint(s), RLX_AGENT);
    }
    if ((wg % GSZ) == 0) {
        const float a0 = wait_granule(gran + wg * C + t, tag);
        const float a1 = wait_granule(gran + wg * C + 768 + t, tag);
        red[t] = a0; red[768 + t] = a1;
        __syncthreads();
        if (t < 96) {
            float s = 0;
#pragma unroll
            for (int r = 0; r < GSZ; ++r) s += red[r * 96 + t];
            __hip_atomic_store((gu64*)(gran + L1_WORDS + (wg / GSZ) * C + t), TAG | __float_as_uint(s), RLX_AGENT);
        }
        __syncthreads();
    }
    {
        const float a0 = wait_granule(gran + L1_WORDS + t, tag);
        const float a1 = wait_granule(gran + L1_WORDS + 768 + t, tag);
        red[t] = a0; red[768 + t] = a1;
    }
    __syncthreads();
    if (wg == 0 && t == 0) __hip_atomic_store((gu64*)epoch_p, (u64)tag, RLX_AGENT);
    if (wave < R) {
        float s0 = 0, s1 = 0;
#pragma unroll
        for (int r = 0; r < NGRP; ++r) { s0 += red[r * 96 + lane]; if (lane < 32) s1 += red[r * 96 + 64 + lane]; }
        float acc = rbf(s0 * (1.0f / NPIX)) * wd0 + rbf(s1 * (1.0f / NPIX)) * wd1;
#pragma unroll
        for (int m = 32; m >= 1; m >>= 1) acc += __shfl_xor(acc, m);
        if (lane == 0) {
            float hp = rbf(acc);
            sh_h[wave] = rbf(hp / (1.0f + __expf(-hp)));
        }
    }
    __syncthreads();
    float g[8];
    {
        const int c0 = (t % 12) * 8;
#pragma unroll
        for (int j = 0; j < 8; ++j) {
            float acc = 0;
#pragma unroll
            for (int r = 0; r < R; ++r) acc += sh_h[r] * sh_wu[r * C + c0 + j];
            acc = rbf(acc);
            g[j] = rbf(1.0f / (1.0f + __expf(-acc)));
        }
    }
    const __amdgpu_buffer_rsrc_t ors = __builtin_amdgcn_make_buffer_rsrc((void*)out, 0, NPIX * C * 2, 0x00020000);
    const unsigned ooff = (wg * NT * CH + t) * 16;
#pragma unroll
    for (int k = 0; k < CH; ++k) {
        u32x4 r;
#pragma unroll
        for (int j = 0; j < 4; ++j) r[j] = pack2(bf_lo(v[k][j]) * g[2 * j], bf_hi(v[k][j]) * g[2 * j + 1]);
        __builtin_amdgcn_raw_buffer_store_b128(r, ors, ooff + k * NT * 16, 0, 16);
    }
}

extern "C" void kernel_launch(void* const* d_in, const int* in_sizes, int n_in,
                              void* d_out, int out_size, void* d_ws, size_t ws_size,
                              hipStream_t stream) {
    const u32x4* x = (const u32x4*)d_in[0];
    const __hip_bfloat16* wd = (const __hip_bfloat16*)d_in[1];
    const __hip_bfloat16* wu = (const __hip_bfloat16*)d_in[2];
    u64* gran = (u64*)d_ws;
    se_fused<<<NWG, NT, 0, stream>>>(x, gran, wd, wu, (u32x4*)d_out);
}
```

```cpp
#include <hip/hip_runtime.h>
#include <hip/hip_bf16.h>
#include <stdint.h>

typedef unsigned int u32;
typedef unsigned long long u64;
typedef u32 u32x4 __attribute__((ext_vector_type(4)));
typedef float f32x4 __attribute__((ext_vector_type(4)));
#define GAS __attribute__((address_space(1)))
typedef GAS u64 gu64;
#define RLX_AGENT __ATOMIC_RELAXED, __HIP_MEMORY_SCOPE_AGENT

constexpr int C = 96, R = 6;
constexpr int NPIX = 512 * 512;
constexpr int NT = 768;
constexpr int NWG = 256;
constexpr int CH = 16;
static_assert((long)NWG * NT * CH * 8 == (long)NPIX * C, "tiling");
constexpr int GSZ = 16;
constexpr int NGRP = NWG / GSZ;
constexpr int L1_WORDS = NWG * C;
constexpr int L2_WORDS = NGRP * C;
constexpr int CTL_BYTES = (L1_WORDS + L2_WORDS) * 8;
constexpr unsigned kSpinCap = 1u << 20;

__device__ __forceinline__ float bf_lo(u32 v) { return __uint_as_float(v << 16); }
__device__ __forceinline__ float bf_hi(u32 v) { return __uint_as_float(v & 0xffff0000u); }
__device__ __forceinline__ float rbf(float f) { return __bfloat162float(__float2bfloat16(f)); }
__device__ __forceinline__ u32 pack2(float a, float b) {
    return (u32)__bfloat16_as_ushort(__float2bfloat16(a)) | ((u32)__bfloat16_as_ushort(__float2bfloat16(b)) << 16);
}
__device__ __forceinline__ float wait_granule(u64* p, u32 tag) {
    u64 x;
    for (unsigned spins = 0;; ) {
        x = __hip_atomic_load((gu64*)p, RLX_AGENT);
        if ((u32)(x >> 32) == tag) break;
        __builtin_amdgcn_s_sleep(1);
        if (++spins > kSpinCap) break;
    }
    return __uint_as_float((u32)x);
}

__global__ void __launch_bounds__(NT, 3) se_fused(const u32x4* __restrict__ x, u64* gran,
                                                  const __hip_bfloat16* __restrict__ wd, const __hip_bfloat16* __restrict__ wu,
                                                  u32x4* __restrict__ out) {
    __shared__ float red[NT * 8];
    __shared__ float red2[4 * 96];
    __shared__ float sh_wu[R * C];
    __shared__ float sh_h[8];
    const int t = threadIdx.x, wg = blockIdx.x;
    const int wave = t >> 6, lane = t & 63;
    u64* epoch_p = gran + L1_WORDS + L2_WORDS;
    const u32 tag = (u32)__hip_atomic_load((gu64*)epoch_p, RLX_AGENT) + 1u;
    const u64 TAG = (u64)tag << 32;
    const __amdgpu_buffer_rsrc_t xrs = __builtin_amdgcn_make_buffer_rsrc((void*)x, 0, NPIX * C * 2, 0x00020000);
    u32x4 v[CH];
    {
        const unsigned xoff = (wg * NT * CH + t) * 16;
#pragma unroll
        for (int k = 0; k < CH; ++k) v[k] = __builtin_bit_cast(u32x4, __builtin_amdgcn_raw_buffer_load_b128(xrs, xoff + k * NT * 16, 0, 0));
    }
    float wd0 = 0.f, wd1 = 0.f;
    if (wave < R) {
        wd0 = __bfloat162float(wd[lane * R + wave]);
        if (lane < 32) wd1 = __bfloat162float(wd[(64 + lane) * R + wave]);
    }
    if (t < R * C) sh_wu[t] = __bfloat162float(wu[t]);
    {
        float a[8] = {0, 0, 0, 0, 0, 0, 0, 0};
#pragma unroll
        for (int k = 0; k < CH; ++k) {
#pragma unroll
            for (int j = 0; j < 4; ++j) { a[2 * j] += bf_lo(v[k][j]); a[2 * j + 1] += bf_hi(v[k][j]); }
        }
        *(f32x4*)&red[t * 8] = (f32x4){a[0], a[1], a[2], a[3]};
        *(f32x4*)&red[t * 8 + 4] = (f32x4){a[4], a[5], a[6], a[7]};
    }
#pragma unroll
    for (int k = 0; k < CH; ++k) asm volatile("" : "+v"(v[k]));
    __syncthreads();
    if (t < 384) {
        const int c = t % 96, q = t / 96;
        float s = 0;
#pragma unroll
        for (int r = 0; r < 16; ++r) s += red[(q * 16 + r) * 96 + c];
        red2[q * 96 + c] = s;
    }
    __syncthreads();
    if (t < 96) {
        const float s = (red2[t] + red2[96 + t]) + (red2[192 + t] + red2[288 + t]);
        __hip_atomic_store((gu64*)(gran + wg * C + t), TAG | __float_as_uint(s), RLX_AGENT);
    }
    if ((wg % GSZ) == 0) {
        const float a0 = wait_granule(gran + wg * C + t, tag);
        const float a1 = wait_granule(gran + wg * C + 768 + t, tag);
        red[t] = a0; red[768 + t] = a1;
        __syncthreads();
        if (t < 96) {
            float s = 0;
#pragma unroll
            for (int r = 0; r < GSZ; ++r) s += red[r * 96 + t];
            __hip_atomic_store((gu64*)(gran + L1_WORDS + (wg / GSZ) * C + t), TAG | __float_as_uint(s), RLX_AGENT);
        }
        __syncthreads();
    }
    {
        const float a0 = wait_granule(gran + L1_WORDS + t, tag);
        const float a1 = wait_granule(gran + L1_WORDS + 768 + t, tag);
        red[t] = a0; red[768 + t] = a1;
    }
    __syncthreads();
    if (wg == 0 && t == 0) __hip_atomic_store((gu64*)epoch_p, (u64)tag, RLX_AGENT);
    if (wave < R) {
        float s0 = 0, s1 = 0;
#pragma unroll
        for (int r = 0; r < NGRP; ++r) { s0 += red[r * 96 + lane]; if (lane < 32) s1 += red[r * 96 + 64 + lane]; }
        float acc = rbf(s0 * (1.0f / NPIX)) * wd0 + rbf(s1 * (1.0f / NPIX)) * wd1;
#pragma unroll
        for (int m = 32; m >= 1; m >>= 1) acc += __shfl_xor(acc, m);
        if (lane == 0) {
            float hp = rbf(acc);
            sh_h[wave] = rbf(hp / (1.0f + __expf(-hp)));
        }
    }
    __syncthreads();
    float g[8];
    {
        const int c0 = (t % 12) * 8;
#pragma unroll
        for (int j = 0; j < 8; ++j) {
            float acc = 0;
#pragma unroll
            for (int r = 0; r < R; ++r) acc += sh_h[r] * sh_wu[r * C + c0 + j];
            acc = rbf(acc);
            g[j] = rbf(1.0f / (1.0f + __expf(-acc)));
        }
    }
    const __amdgpu_buffer_rsrc_t ors = __builtin_amdgcn_make_buffer_rsrc((void*)out, 0, NPIX * C * 2, 0x00020000);
    const unsigned ooff = (wg * NT * CH + t) * 16;
#pragma unroll
    for (int k = 0; k < CH; ++k) {
        u32x4 r;
#pragma unroll
        for (int j = 0; j < 4; ++j) r[j] = pack2(bf_lo(v[k][j]) * g[2 * j], bf_hi(v[k][j]) * g[2 * j + 1]);
        __builtin_amdgcn_raw_buffer_store_b128(r, ors, ooff + k * NT * 16, 0, 16);
    }
}

__global__ void __launch_bounds__(NT, 3) se_reduce(const u32x4* __restrict__ x, float* __restrict__ part) {
    __shared__ float red[NT * 8];
    const int t = threadIdx.x, wg = blockIdx.x;
    const u32x4* p = x + (size_t)wg * (NT * CH) + t;
    u32x4 v[CH];
#pragma unroll
    for (int k = 0; k < CH; ++k) v[k] = p[k * NT];
    float a[8] = {0, 0, 0, 0, 0, 0, 0, 0};
#pragma unroll
    for (int k = 0; k < CH; ++k) {
#pragma unroll
        for (int j = 0; j < 4; ++j) { a[2 * j] += bf_lo(v[k][j]); a[2 * j + 1] += bf_hi(v[k][j]); }
    }
    *(f32x4*)&red[t * 8] = (f32x4){a[0], a[1], a[2], a[3]};
    *(f32x4*)&red[t * 8 + 4] = (f32x4){a[4], a[5], a[6], a[7]};
    __syncthreads();
    __shared__ float red2[4 * 96];
    if (t < 384) {
        const int c = t % 96, q = t / 96;
        float s = 0;
#pragma unroll
        for (int r = 0; r < 16; ++r) s += red[(q * 16 + r) * 96 + c];
        red2[q * 96 + c] = s;
    }
    __syncthreads();
    if (t < 96) part[wg * 96 + t] = (red2[t] + red2[96 + t]) + (red2[192 + t] + red2[288 + t]);
}

__global__ void __launch_bounds__(NT, 3) se_scale(const u32x4* __restrict__ x, const float* __restrict__ part,
                                                  const __hip_bfloat16* __restrict__ wd, const __hip_bfloat16* __restrict__ wu,
                                                  u32x4* __restrict__ out) {
    __shared__ float red[32 * 96];
    __shared__ float sh_s[96];
    __shared__ float sh_h[8];
    const int t = threadIdx.x, wg = blockIdx.x;
    f32x4 ps[8];
    {
        const f32x4* pp = (const f32x4*)part + (t / 24) * 8 * 24 + (t % 24);
#pragma unroll
        for (int k = 0; k < 8; ++k) ps[k] = pp[k * 24];
    }
    const u32x4* p = x + (size_t)wg * (NT * CH) + t;
    u32x4 v[CH];
#pragma unroll
    for (int k = 0; k < CH; ++k) v[k] = p[k * NT];
    {
        f32x4 s = ((ps[0] + ps[1]) + (ps[2] + ps[3])) + ((ps[4] + ps[5]) + (ps[6] + ps[7]));
        *(f32x4*)&red[(t / 24) * 96 + (t % 24) * 4] = s;
    }
    __syncthreads();
    if (t < 96) {
        float s = 0;
#pragma unroll
        for (int r = 0; r < 32; ++r) s += red[r * 96 + t];
        sh_s[t] = rbf(s * (1.0f / NPIX));
    }
    __syncthreads();
    const int wave = t >> 6, lane = t & 63;
    if (wave < R) {
        float acc = sh_s[lane] * __bfloat162float(wd[lane * R + wave]);
        if (lane < 32) acc += sh_s[64 + lane] * __bfloat162float(wd[(64 + lane) * R + wave]);
#pragma unroll
        for (int m = 32; m >= 1; m >>= 1) acc += __shfl_xor(acc, m);
        if (lane == 0) {
            float hp = rbf(acc);
            sh_h[wave] = rbf(hp / (1.0f + __expf(-hp)));
        }
    }
    __syncthreads();
    float g[8];
    {
        const int c0 = (t % 12) * 8;
#pragma unroll
        for (int j = 0; j < 8; ++j) {
            float acc = 0;
#pragma unroll
            for (int r = 0; r < R; ++r) acc += sh_h[r] * __bfloat162float(wu[r * C + c0 + j]);
            acc = rbf(acc);
            g[j] = rbf(1.0f / (1.0f + __expf(-acc)));
        }
    }
    u32x4* o = out + (size_t)wg * (NT * CH) + t;
#pragma unroll
    for (int k = 0; k < CH; ++k) {
        u32x4 r;
#pragma unroll
        for (int j = 0; j < 4; ++j) r[j] = pack2(bf_lo(v[k][j]) * g[2 * j], bf_hi(v[k][j]) * g[2 * j + 1]);
        o[k * NT] = r;
    }
}

extern "C" void kernel_launch(void* const* d_in, const int* in_sizes, int n_in,
                              void* d_out, int out_size, void* d_ws, size_t ws_size,
                              hipStream_t stream) {
    const u32x4* x = (const u32x4*)d_in[0];
    const __hip_bfloat16* wd = (const __hip_bfloat16*)d_in[1];
    const __hip_bfloat16* wu = (const __hip_bfloat16*)d_in[2];
    u64* gran = (u64*)d_ws;
    int dev = 0, cus = 0;
    hipGetDevice(&dev);
    hipDeviceGetAttribute(&cus, hipDeviceAttributeMultiprocessorCount, dev);
    if (cus >= NWG) {
        se_fused<<<NWG, NT, 0, stream>>>(x, gran, wd, wu, (u32x4*)d_out);
    } else {
        float* part = (float*)d_ws;
        se_reduce<<<NWG, NT, 0, stream>>>(x, part);
        se_scale<<<NWG, NT, 0, stream>>>(x, part, wd, wu, (u32x4*)d_out);
    }
}
```
